```python
import torch

def _softmax(x, axis=-1):
    return torch.softmax(x, dim=axis)
def _gelu(x):
    return torch.nn.functional.gelu(x, approximate="tanh")
def _silu(x):
    return torch.nn.functional.silu(x)
def _sigmoid(x):
    return torch.sigmoid(x)
def _layernorm(x, axis=-1, eps=1e-5):
    m = x.mean(dim=axis, keepdim=True)
    v = x.var(dim=axis, keepdim=True, unbiased=False)
    return (x - m) / torch.sqrt(v + eps)
def _scatter_add(tgt, idx, src, axis=0):
    return tgt.clone().index_add_(axis, idx, src)
def _segment_sum(src, seg, n):
    out = torch.zeros((n,) + src.shape[1:], dtype=src.dtype)
    return out.index_add_(0, seg, src)
def _pad(x, pw):
    flat = tuple(v for pair in reversed(pw) for v in pair)
    return torch.nn.functional.pad(x, flat)

kernel_name = 'standalone__glu_gated_glu__decode'

def setup_inputs():

    _w = (lambda a: (lambda t: t.to(torch.bfloat16) if t.is_floating_point() else t)(torch.as_tensor(a)))
    B, S, D, F = 4, 1, 1024, 3584
    return {
        "x": _w((lambda *s: torch.randn(*s, dtype=torch.bfloat16) * 0.1)(B, S, D)),
        "w_gate": _w((lambda *s: torch.randn(*s, dtype=torch.bfloat16) * 0.1)(D, F)),
        "w_up": _w((lambda *s: torch.randn(*s, dtype=torch.bfloat16) * 0.05)(D, F)),
        "w_down": _w((lambda *s: torch.randn(*s, dtype=torch.bfloat16) * 0.05)(F, D)),
    }

def reference(x, w_gate, w_up, w_down):
    g = torch.einsum("bsd,df->bsf", x, w_gate)
    u = torch.einsum("bsd,df->bsf", x, w_up)
    h = _sigmoid(g) * u
    return torch.einsum("bsf,fd->bsd", h, w_down)
```

```cpp
#include <hip/hip_runtime.h>
#include <hip/hip_bf16.h>
#include <stdint.h>

typedef unsigned v4u __attribute__((ext_vector_type(4)));
typedef unsigned v2u __attribute__((ext_vector_type(2)));
typedef float f32x4 __attribute__((ext_vector_type(4)));
typedef short bf16x8 __attribute__((ext_vector_type(8)));
typedef short bf16x4 __attribute__((ext_vector_type(4)));

constexpr int D = 1024, F = 3584, NB = 4;
constexpr int NSL = 56;

__device__ __forceinline__ unsigned f2bf(float f) {
  __hip_bfloat16 b = __float2bfloat16(f);
  return (unsigned)__builtin_bit_cast(unsigned short, b);
}
__device__ __forceinline__ float bf2f(unsigned b) { return __builtin_bit_cast(float, b << 16); }
__device__ __forceinline__ float rbf(float f) { return bf2f(f2bf(f)); }

__global__ void __launch_bounds__(512, 2) k_gu(const unsigned short* x, const unsigned short* wg, const unsigned short* wu, float* part) {
  __shared__ float red[8 * 512];
  const int id = blockIdx.x, s = (id & 7) + 8 * (id >> 5), q = (id >> 3) & 3;
  const int tid = threadIdx.x, l = tid & 63, w = tid >> 6;
  const int np = l & 15, kg = l >> 4;
  const int r0 = 256 * q + 32 * w + 8 * kg;
  const unsigned short* W = (np & 8) ? wu : wg;
  const unsigned short* p = W + (size_t)r0 * F + 64 * s + 8 * (np & 7);
  v4u a = *(const v4u*)(x + (np & 3) * D + r0);
  v4u R[8];
#pragma unroll
  for (int i = 0; i < 8; ++i) R[i] = __builtin_nontemporal_load((const v4u*)(p + (size_t)i * F));
  f32x4 acc[8];
#pragma unroll
  for (int j = 0; j < 8; ++j) {
    const int d = j >> 1;
    const unsigned sel = (j & 1) ? 0x07060302u : 0x05040100u;
    v4u b;
#pragma unroll
    for (int pp = 0; pp < 4; ++pp) b[pp] = __builtin_amdgcn_perm(R[2 * pp + 1][d], R[2 * pp][d], sel);
    f32x4 z = {0.f, 0.f, 0.f, 0.f};
    acc[j] = __builtin_amdgcn_mfma_f32_16x16x32_bf16(__builtin_bit_cast(bf16x8, a), __builtin_bit_cast(bf16x8, b), z, 0, 0, 0);
  }
#pragma unroll
  for (int jj = 0; jj < 2; ++jj) {
    f32x4 v = kg == 0 ? acc[jj] : kg == 1 ? acc[2 + jj] : kg == 2 ? acc[4 + jj] : acc[6 + jj];
    *(f32x4*)&red[w * 512 + (np * 8 + 2 * kg + jj) * 4] = v;
  }
  __syncthreads();
  float sum = 0.f;
#pragma unroll
  for (int ww = 0; ww < 8; ++ww) sum += red[ww * 512 + tid];
  part[(size_t)(s * 4 + q) * 512 + tid] = sum;
}

__global__ void __launch_bounds__(512, 2) k_dn(const unsigned short* wd, const float* part, float* part2) {
  __shared__ float red[8 * 512];
  __shared__ __attribute__((aligned(16))) unsigned short hs[4 * 64];
  const int id = blockIdx.x, s = (id & 7) + 8 * (id >> 5), n = (id >> 3) & 3;
  const int tid = threadIdx.x, l = tid & 63, w = tid >> 6;
  const int np = l & 15, kg = l >> 4;
  const int ch = w & 1, kq = w >> 1;
  const int r0 = 64 * s + 16 * kq + 4 * kg;
  const unsigned short* p = wd + (size_t)r0 * D + 256 * n + 128 * ch + 8 * np;
  v4u R[4];
#pragma unroll
  for (int i = 0; i < 4; ++i) R[i] = __builtin_nontemporal_load((const v4u*)(p + (size_t)i * D));
  if (tid < 256) {
    const int col = tid >> 2, b = tid & 3;
    float g = 0.f, u = 0.f;
#pragma unroll
    for (int qq = 0; qq < 4; ++qq) {
      g += part[(size_t)(s * 4 + qq) * 512 + tid];
      u += part[(size_t)(s * 4 + qq) * 512 + 256 + tid];
    }
    g = rbf(g); u = rbf(u);
    float sg = rbf(1.0f / (1.0f + __expf(-g)));
    hs[b * 64 + col] = (unsigned short)f2bf(sg * u);
  }
  __syncthreads();
  v2u a = *(const v2u*)&hs[(np & 3) * 64 + 16 * kq + 4 * kg];
  f32x4 acc[8];
#pragma unroll
  for (int j = 0; j < 8; ++j) {
    const int d = j >> 1;
    const unsigned sel = (j & 1) ? 0x07060302u : 0x05040100u;
    v2u b;
#pragma unroll
    for (int pp = 0; pp < 2; ++pp) b[pp] = __builtin_amdgcn_perm(R[2 * pp + 1][d], R[2 * pp][d], sel);
    f32x4 z = {0.f, 0.f, 0.f, 0.f};
    acc[j] = __builtin_amdgcn_mfma_f32_16x16x16bf16_1k(__builtin_bit_cast(bf16x4, a), __builtin_bit_cast(bf16x4, b), z, 0, 0, 0);
  }
#pragma unroll
  for (int jj = 0; jj < 2; ++jj) {
    f32x4 v = kg == 0 ? acc[jj] : kg == 1 ? acc[2 + jj] : kg == 2 ? acc[4 + jj] : acc[6 + jj];
    *(f32x4*)&red[w * 512 + (np * 8 + 2 * kg + jj) * 4] = v;
  }
  __syncthreads();
#pragma unroll
  for (int h = 0; h < 2; ++h) {
    float sum = 0.f;
#pragma unroll
    for (int k = 0; k < 4; ++k) sum += red[(k * 2 + h) * 512 + tid];
    const int col = 256 * n + 128 * h + (tid >> 2), b = tid & 3;
    part2[(size_t)s * 4096 + b * D + col] = sum;
  }
}

__global__ void __launch_bounds__(256) k_red(const float* part2, unsigned short* out) {
  const int i = blockIdx.x * 256 + threadIdx.x;
  float sum = 0.f;
#pragma unroll 8
  for (int s = 0; s < NSL; ++s) sum += part2[(size_t)s * 4096 + i];
  out[i] = (unsigned short)f2bf(sum);
}

extern "C" void kernel_launch(void* const* d_in, const int* in_sizes, int n_in, void* d_out, int out_size, void* d_ws, size_t ws_size,
                              hipStream_t stream) {
  const unsigned short* x = (const unsigned short*)d_in[0];
  const unsigned short* wg = (const unsigned short*)d_in[1];
  const unsigned short* wu = (const unsigned short*)d_in[2];
  const unsigned short* wd = (const unsigned short*)d_in[3];
  float* part = (float*)d_ws;
  float* part2 = part + 224 * 512;
  k_gu<<<224, 512, 0, stream>>>(x, wg, wu, part);
  k_dn<<<224, 512, 0, stream>>>(wd, part, part2);
  k_red<<<16, 256, 0, stream>>>(part2, (unsigned short*)d_out);
}
```

```cpp
#include <hip/hip_runtime.h>
#include <hip/hip_bf16.h>
#include <stdint.h>

typedef unsigned v4u __attribute__((ext_vector_type(4)));
typedef unsigned v2u __attribute__((ext_vector_type(2)));
typedef float f32x4 __attribute__((ext_vector_type(4)));
typedef float f32x2 __attribute__((ext_vector_type(2)));
typedef short bf16x8 __attribute__((ext_vector_type(8)));
typedef short bf16x4 __attribute__((ext_vector_type(4)));
#define GAS __attribute__((address_space(1)))
#define RLX_AGENT __ATOMIC_RELAXED, __HIP_MEMORY_SCOPE_AGENT

constexpr int D = 1024, F = 3584;
constexpr int NSL = 56, NWG = 224;
constexpr int H1_OFF = 4096, H1_BYTES = NSL * 4 * 256 * 16;
constexpr int H2_OFF = H1_OFF + H1_BYTES, H2_BYTES = 256 * NSL * 8 * 16;
constexpr int WS_BYTES = H2_OFF + H2_BYTES;
constexpr int AUX_SC1 = 16;
constexpr unsigned SPIN_CAP = 1u << 18;

__device__ __forceinline__ unsigned f2bf(float f) {
  __hip_bfloat16 b = __float2bfloat16(f);
  return (unsigned)__builtin_bit_cast(unsigned short, b);
}
__device__ __forceinline__ float bf2f(unsigned b) { return __builtin_bit_cast(float, b << 16); }
__device__ __forceinline__ float rbf(float f) { return bf2f(f2bf(f)); }
__device__ __forceinline__ float asf(unsigned u) { return __builtin_bit_cast(float, u); }
__device__ __forceinline__ unsigned asu(float f) { return __builtin_bit_cast(unsigned, f); }

__global__ void __launch_bounds__(512, 2)
glu_fused(const unsigned short* __restrict__ x, const unsigned short* __restrict__ wg, const unsigned short* __restrict__ wu,
          const unsigned short* __restrict__ wd, unsigned* __restrict__ out, unsigned char* ws) {
  __shared__ __attribute__((aligned(16))) float red[8 * 512];
  __shared__ __attribute__((aligned(16))) unsigned short hs[4 * 64];
  const int id = blockIdx.x, s = (id & 7) + 8 * (id >> 5), q = (id >> 3) & 3;
  const int tid = threadIdx.x, l = tid & 63, w = __builtin_amdgcn_readfirstlane(tid >> 6);
  const int np = l & 15, kg = l >> 4;
  const __amdgpu_buffer_rsrc_t rs = __builtin_amdgcn_make_buffer_rsrc((void*)ws, 0, WS_BYTES, 0x00020000);

  const unsigned long long ep = __hip_atomic_load((GAS unsigned long long*)ws, RLX_AGENT);
  const int r0 = 256 * q + 32 * w + 8 * kg;
  const v4u xa = *(const v4u*)(x + (np & 3) * D + r0);
  const unsigned short* p1 = ((np & 8) ? wu : wg) + (size_t)r0 * F + 64 * s + 8 * (np & 7);
  v4u R[8];
#pragma unroll
  for (int i = 0; i < 8; ++i) R[i] = __builtin_nontemporal_load((const v4u*)(p1 + (size_t)i * F));
  const int ch = w & 1, kq = w >> 1;
  const unsigned short* p2 = wd + (size_t)(64 * s + 16 * kq + 4 * kg) * D + 256 * q + 128 * ch + 8 * np;
  v4u R2[4];
#pragma unroll
  for (int i = 0; i < 4; ++i) R2[i] = __builtin_nontemporal_load((const v4u*)(p2 + (size_t)i * D));
  __builtin_amdgcn_sched_barrier(0);

  const unsigned long long ep1 = ep + 0x9E3779B97F4A7C15ull;
  const unsigned tagA = __builtin_amdgcn_readfirstlane((unsigned)ep1), tagB = __builtin_amdgcn_readfirstlane((unsigned)(ep1 >> 32));

  {
    f32x4 acc0 = {0.f, 0.f, 0.f, 0.f}, acc1 = acc0;
#pragma unroll
    for (int r = 0; r < 4; ++r) {
      const bool on = (np >> 2) == r;
      v4u a;
#pragma unroll
      for (int e = 0; e < 4; ++e) a[e] = on ? xa[e] : 0u;
      v4u b0, b1;
#pragma unroll
      for (int pp = 0; pp < 4; ++pp) {
        b0[pp] = __builtin_amdgcn_perm(R[2 * pp + 1][r], R[2 * pp][r], 0x05040100u);
        b1[pp] = __builtin_amdgcn_perm(R[2 * pp + 1][r], R[2 * pp][r], 0x07060302u);
      }
      acc0 = __builtin_amdgcn_mfma_f32_16x16x32_bf16(__builtin_bit_cast(bf16x8, a), __builtin_bit_cast(bf16x8, b0), acc0, 0, 0, 0);
      acc1 = __builtin_amdgcn_mfma_f32_16x16x32_bf16(__builtin_bit_cast(bf16x8, a), __builtin_bit_cast(bf16x8, b1), acc1, 0, 0, 0);
    }
    const int c0 = np * 8 + 2 * kg;
    *(f32x4*)&red[w * 512 + c0 * 4] = acc0;
    *(f32x4*)&red[w * 512 + c0 * 4 + 4] = acc1;
  }
  __syncthreads();
  if (w < 4) {
    float g = 0.f, u = 0.f;
#pragma unroll
    for (int ww = 0; ww < 8; ++ww) {
      g += red[ww * 512 + tid];
      u += red[ww * 512 + 256 + tid];
    }
    const v4u pub = {asu(g), tagA, asu(u), tagB};
    __builtin_amdgcn_raw_buffer_store_b128(pub, rs, H1_OFF + ((s * 4 + q) * 256 + tid) * 16, 0, AUX_SC1);
    v4u o[4];
    for (unsigned spins = 0; spins < SPIN_CAP; ++spins) {
      asm volatile("" ::: "memory");
      bool ok = true;
#pragma unroll
      for (int k = 0; k < 4; ++k) {
        o[k] = __builtin_amdgcn_raw_buffer_load_b128(rs, H1_OFF + ((s * 4 + k) * 256 + tid) * 16, 0, AUX_SC1);
        ok = ok && o[k][1] == tagA && o[k][3] == tagB;
      }
      if (__all(ok)) break;
    }
    float gs = rbf((asf(o[0][0]) + asf(o[1][0])) + (asf(o[2][0]) + asf(o[3][0])));
    float us = rbf((asf(o[0][2]) + asf(o[1][2])) + (asf(o[2][2]) + asf(o[3][2])));
    const float sg = rbf(1.0f / (1.0f + __expf(-gs)));
    hs[(tid & 3) * 64 + (tid >> 2)] = (unsigned short)f2bf(sg * us);
  }
  __syncthreads();
  {
    const v2u ha = *(const v2u*)&hs[(np & 3) * 64 + 16 * kq + 4 * kg];
    f32x4 acc0 = {0.f, 0.f, 0.f, 0.f}, acc1 = acc0;
#pragma unroll
    for (int r = 0; r < 4; ++r) {
      const bool on = (np >> 2) == r;
      v2u a;
      a[0] = on ? ha[0] : 0u;
      a[1] = on ? ha[1] : 0u;
      v2u b0, b1;
#pragma unroll
      for (int pp = 0; pp < 2; ++pp) {
        b0[pp] = __builtin_amdgcn_perm(R2[2 * pp + 1][r], R2[2 * pp][r], 0x05040100u);
        b1[pp] = __builtin_amdgcn_perm(R2[2 * pp + 1][r], R2[2 * pp][r], 0x07060302u);
      }
      acc0 = __builtin_amdgcn_mfma_f32_16x16x16bf16_1k(__builtin_bit_cast(bf16x4, a), __builtin_bit_cast(bf16x4, b0), acc0, 0, 0, 0);
      acc1 = __builtin_amdgcn_mfma_f32_16x16x16bf16_1k(__builtin_bit_cast(bf16x4, a), __builtin_bit_cast(bf16x4, b1), acc1, 0, 0, 0);
    }
    const int c0 = np * 8 + 2 * kg;
#pragma unroll
    for (int b = 0; b < 4; ++b) {
      f32x2 v = {acc0[b], acc1[b]};
      *(f32x2*)&red[w * 512 + b * 128 + c0] = v;
    }
  }
  __syncthreads();
  {
    const int b = tid >> 7, cpl = tid & 127, c2 = cpl >> 6;
    f32x2 sum = {0.f, 0.f};
#pragma unroll
    for (int k = 0; k < 4; ++k) sum += *(const f32x2*)&red[(k * 2 + c2) * 512 + b * 128 + 2 * (cpl & 63)];
    const int r = b * 64 + 16 * q + (cpl >> 3);
    const v4u pub = {asu(sum[0]), tagA, asu(sum[1]), tagB};
    __builtin_amdgcn_raw_buffer_store_b128(pub, rs, H2_OFF + ((r * NSL + s) * 8 + (cpl & 7)) * 16, 0, AUX_SC1);
  }
  if (w == 0 || (w == 1 && id < 32)) {
    const int r = id + NWG * w;
    v4u o[7];
    for (unsigned spins = 0; spins < SPIN_CAP; ++spins) {
      asm volatile("" ::: "memory");
      bool ok = true;
#pragma unroll
      for (int k = 0; k < 7; ++k) {
        o[k] = __builtin_amdgcn_raw_buffer_load_b128(rs, H2_OFF + (r * NSL * 8 + k * 64 + l) * 16, 0, AUX_SC1);
        ok = ok && o[k][1] == tagA && o[k][3] == tagB;
      }
      if (__all(ok)) break;
    }
    float s0 = 0.f, s1 = 0.f;
#pragma unroll
    for (int k = 0; k < 7; ++k) { s0 += asf(o[k][0]); s1 += asf(o[k][2]); }
#pragma unroll
    for (int m = 8; m < 64; m <<= 1) { s0 += __shfl_xor(s0, m); s1 += __shfl_xor(s1, m); }
    if (l < 8) out[r * 8 + l] = f2bf(s0) | (f2bf(s1) << 16);
    if (r == 0 && l == 0) __hip_atomic_store((GAS unsigned long long*)ws, ep1, RLX_AGENT);
  }
}

extern "C" void kernel_launch(void* const* d_in, const int* in_sizes, int n_in, void* d_out, int out_size, void* d_ws, size_t ws_size,
                              hipStream_t stream) {
  glu_fused<<<NWG, 512, 0, stream>>>((const unsigned short*)d_in[0], (const unsigned short*)d_in[1], (const unsigned short*)d_in[2],
                                     (const unsigned short*)d_in[3], (unsigned*)d_out, (unsigned char*)d_ws);
}
```

```cpp
#define PROBE_K 6
#define PROBE_M 0
#include <hip/hip_runtime.h>
#include <hip/hip_bf16.h>
#include <stdint.h>

typedef unsigned v4u __attribute__((ext_vector_type(4)));
typedef unsigned v2u __attribute__((ext_vector_type(2)));
typedef float f32x4 __attribute__((ext_vector_type(4)));
typedef float f32x2 __attribute__((ext_vector_type(2)));
typedef short bf16x8 __attribute__((ext_vector_type(8)));
typedef short bf16x4 __attribute__((ext_vector_type(4)));
#define GAS __attribute__((address_space(1)))
#define RLX_AGENT __ATOMIC_RELAXED, __HIP_MEMORY_SCOPE_AGENT

constexpr int D = 1024, F = 3584;
constexpr int NSL = 56, NWG = 224;
constexpr int H1_OFF = 4096, H1_BYTES = NSL * 4 * 256 * 16;
constexpr int H2_OFF = H1_OFF + H1_BYTES, H2_BYTES = 256 * NSL * 8 * 16;
constexpr int WS_BYTES = H2_OFF + H2_BYTES;
constexpr int AUX_SC1 = 16;
constexpr unsigned SPIN_CAP = 1u << 18;
#define STAMP(k) do { if ((k) == 0 || (k) == PROBE_K) ts[k] = __builtin_amdgcn_s_memrealtime(); } while (0)

__device__ __forceinline__ unsigned f2bf(float f) {
  __hip_bfloat16 b = __float2bfloat16(f);
  return (unsigned)__builtin_bit_cast(unsigned short, b);
}
__device__ __forceinline__ float bf2f(unsigned b) { return __builtin_bit_cast(float, b << 16); }
__device__ __forceinline__ float rbf(float f) { return bf2f(f2bf(f)); }
__device__ __forceinline__ float asf(unsigned u) { return __builtin_bit_cast(float, u); }
__device__ __forceinline__ unsigned asu(float f) { return __builtin_bit_cast(unsigned, f); }

__global__ void __launch_bounds__(512, 2)
glu_fused(const unsigned short* __restrict__ x, const unsigned short* __restrict__ wg, const unsigned short* __restrict__ wu,
          const unsigned short* __restrict__ wd, unsigned* __restrict__ out, unsigned char* ws) {
  __shared__ __attribute__((aligned(16))) float red[8 * 512];
  __shared__ __attribute__((aligned(16))) unsigned short hs[4 * 64];
  unsigned long long ts[8];
  const int id = blockIdx.x, s = (id & 7) + 8 * (id >> 5), q = (id >> 3) & 3;
  const int tid = threadIdx.x, l = tid & 63, w = __builtin_amdgcn_readfirstlane(tid >> 6);
  const int np = l & 15, kg = l >> 4;
  const __amdgpu_buffer_rsrc_t rs = __builtin_amdgcn_make_buffer_rsrc((void*)ws, 0, WS_BYTES, 0x00020000);

  STAMP(0);
  const unsigned long long ep = __hip_atomic_load((GAS unsigned long long*)ws, RLX_AGENT);
  const int r0 = 256 * q + 32 * w + 8 * kg;
  const v4u xa = *(const v4u*)(x + (np & 3) * D + r0);
  const unsigned short* p1 = ((np & 8) ? wu : wg) + (size_t)r0 * F + 64 * s + 8 * (np & 7);
  v4u R[8];
#pragma unroll
  for (int i = 0; i < 8; ++i) R[i] = __builtin_nontemporal_load((const v4u*)(p1 + (size_t)i * F));
  const int ch = w & 1, kq = w >> 1;
  const unsigned short* p2 = wd + (size_t)(64 * s + 16 * kq + 4 * kg) * D + 256 * q + 128 * ch + 8 * np;
  v4u R2[4];
#pragma unroll
  for (int i = 0; i < 4; ++i) R2[i] = __builtin_nontemporal_load((const v4u*)(p2 + (size_t)i * D));
  __builtin_amdgcn_sched_barrier(0);

  const unsigned long long ep1 = ep + 0x9E3779B97F4A7C15ull;
  const unsigned tagA = __builtin_amdgcn_readfirstlane((unsigned)ep1), tagB = __builtin_amdgcn_readfirstlane((unsigned)(ep1 >> 32));

  STAMP(1);
  {
    f32x4 acc0 = {0.f, 0.f, 0.f, 0.f}, acc1 = acc0;
#pragma unroll
    for (int r = 0; r < 4; ++r) {
      const bool on = (np >> 2) == r;
      v4u a;
#pragma unroll
      for (int e = 0; e < 4; ++e) a[e] = on ? xa[e] : 0u;
      v4u b0, b1;
#pragma unroll
      for (int pp = 0; pp < 4; ++pp) {
        b0[pp] = __builtin_amdgcn_perm(R[2 * pp + 1][r], R[2 * pp][r], 0x05040100u);
        b1[pp] = __builtin_amdgcn_perm(R[2 * pp + 1][r], R[2 * pp][r], 0x07060302u);
      }
      acc0 = __builtin_amdgcn_mfma_f32_16x16x32_bf16(__builtin_bit_cast(bf16x8, a), __builtin_bit_cast(bf16x8, b0), acc0, 0, 0, 0);
      acc1 = __builtin_amdgcn_mfma_f32_16x16x32_bf16(__builtin_bit_cast(bf16x8, a), __builtin_bit_cast(bf16x8, b1), acc1, 0, 0, 0);
    }
    const int c0 = np * 8 + 2 * kg;
    *(f32x4*)&red[w * 512 + c0 * 4] = acc0;
    *(f32x4*)&red[w * 512 + c0 * 4 + 4] = acc1;
  }
  STAMP(2);
  __syncthreads();
  STAMP(3);
  if (w < 4) {
    float g = 0.f, u = 0.f;
#pragma unroll
    for (int ww = 0; ww < 8; ++ww) {
      g += red[ww * 512 + tid];
      u += red[ww * 512 + 256 + tid];
    }
    const v4u pub = {asu(g), tagA, asu(u), tagB};
    __builtin_amdgcn_raw_buffer_store_b128(pub, rs, H1_OFF + ((s * 4 + q) * 256 + tid) * 16, 0, AUX_SC1);
    v4u o[4];
    for (unsigned spins = 0; spins < SPIN_CAP; ++spins) {
      asm volatile("" ::: "memory");
      bool ok = true;
#pragma unroll
      for (int k = 0; k < 4; ++k) {
        o[k] = __builtin_amdgcn_raw_buffer_load_b128(rs, H1_OFF + ((s * 4 + k) * 256 + tid) * 16, 0, AUX_SC1);
        ok = ok && o[k][1] == tagA && o[k][3] == tagB;
      }
      if (__all(ok)) break;
    }
    float gs = rbf((asf(o[0][0]) + asf(o[1][0])) + (asf(o[2][0]) + asf(o[3][0])));
    float us = rbf((asf(o[0][2]) + asf(o[1][2])) + (asf(o[2][2]) + asf(o[3][2])));
    const float sg = rbf(1.0f / (1.0f + __expf(-gs)));
    hs[(tid & 3) * 64 + (tid >> 2)] = (unsigned short)f2bf(sg * us);
  }
  STAMP(4);
  __syncthreads();
  {
    const v2u ha = *(const v2u*)&hs[(np & 3) * 64 + 16 * kq + 4 * kg];
    f32x4 acc0 = {0.f, 0.f, 0.f, 0.f}, acc1 = acc0;
#pragma unroll
    for (int r = 0; r < 4; ++r) {
      const bool on = (np >> 2) == r;
      v2u a;
      a[0] = on ? ha[0] : 0u;
      a[1] = on ? ha[1] : 0u;
      v2u b0, b1;
#pragma unroll
      for (int pp = 0; pp < 2; ++pp) {
        b0[pp] = __builtin_amdgcn_perm(R2[2 * pp + 1][r], R2[2 * pp][r], 0x05040100u);
        b1[pp] = __builtin_amdgcn_perm(R2[2 * pp + 1][r], R2[2 * pp][r], 0x07060302u);
      }
      acc0 = __builtin_amdgcn_mfma_f32_16x16x16bf16_1k(__builtin_bit_cast(bf16x4, a), __builtin_bit_cast(bf16x4, b0), acc0, 0, 0, 0);
      acc1 = __builtin_amdgcn_mfma_f32_16x16x16bf16_1k(__builtin_bit_cast(bf16x4, a), __builtin_bit_cast(bf16x4, b1), acc1, 0, 0, 0);
    }
    const int c0 = np * 8 + 2 * kg;
#pragma unroll
    for (int b = 0; b < 4; ++b) {
      f32x2 v = {acc0[b], acc1[b]};
      *(f32x2*)&red[w * 512 + b * 128 + c0] = v;
    }
  }
  __syncthreads();
  {
    const int b = tid >> 7, cpl = tid & 127, c2 = cpl >> 6;
    f32x2 sum = {0.f, 0.f};
#pragma unroll
    for (int k = 0; k < 4; ++k) sum += *(const f32x2*)&red[(k * 2 + c2) * 512 + b * 128 + 2 * (cpl & 63)];
    const int r = b * 64 + 16 * q + (cpl >> 3);
    const v4u pub = {asu(sum[0]), tagA, asu(sum[1]), tagB};
    __builtin_amdgcn_raw_buffer_store_b128(pub, rs, H2_OFF + ((r * NSL + s) * 8 + (cpl & 7)) * 16, 0, AUX_SC1);
  }
  STAMP(5);
  if (w == 0 || (w == 1 && id < 32)) {
    const int r = id + NWG * w;
    v4u o[7];
    for (unsigned spins = 0; spins < SPIN_CAP; ++spins) {
      asm volatile("" ::: "memory");
      bool ok = true;
#pragma unroll
      for (int k = 0; k < 7; ++k) {
        o[k] = __builtin_amdgcn_raw_buffer_load_b128(rs, H2_OFF + (r * NSL * 8 + k * 64 + l) * 16, 0, AUX_SC1);
        ok = ok && o[k][1] == tagA && o[k][3] == tagB;
      }
      if (__all(ok)) break;
    }
    float s0 = 0.f, s1 = 0.f;
#pragma unroll
    for (int k = 0; k < 7; ++k) { s0 += asf(o[k][0]); s1 += asf(o[k][2]); }
#pragma unroll
    for (int m = 8; m < 64; m <<= 1) { s0 += __shfl_xor(s0, m); s1 += __shfl_xor(s1, m); }
    STAMP(6);
    if (l < 8) out[r * 8 + l] = f2bf(s0) | (f2bf(s1) << 16);
    if (r == 0 && l == 0) __hip_atomic_store((GAS unsigned long long*)ws, ep1, RLX_AGENT);
    if (r == 0) {
      const unsigned long long now = __builtin_amdgcn_s_memrealtime();
      const unsigned long long dt = (ts[PROBE_K] - ts[0]) * PROBE_M;
      while (__builtin_amdgcn_s_memrealtime() - now < dt) __builtin_amdgcn_s_sleep(1);
    }
  }
}

extern "C" void kernel_launch(void* const* d_in, const int* in_sizes, int n_in, void* d_out, int out_size, void* d_ws, size_t ws_size,
                              hipStream_t stream) {
  glu_fused<<<NWG, 512, 0, stream>>>((const unsigned short*)d_in[0], (const unsigned short*)d_in[1], (const unsigned short*)d_in[2],
                                     (const unsigned short*)d_in[3], (unsigned*)d_out, (unsigned char*)d_ws);
}
```

```cpp
#include <hip/hip_runtime.h>
#include <hip/hip_bf16.h>
#include <stdint.h>

typedef unsigned v4u __attribute__((ext_vector_type(4)));
typedef unsigned v2u __attribute__((ext_vector_type(2)));
typedef float f32x4 __attribute__((ext_vector_type(4)));
typedef float f32x2 __attribute__((ext_vector_type(2)));
typedef short bf16x8 __attribute__((ext_vector_type(8)));
#define GAS __attribute__((address_space(1)))
#define RLX_AGENT __ATOMIC_RELAXED, __HIP_MEMORY_SCOPE_AGENT

constexpr int D = 1024, F = 3584;
constexpr int NSL = 56, NWG = 224, NTHR = 768;
constexpr int H1_OFF = 4096, H1_BYTES = NSL * 4 * 128 * 16;
constexpr int H2_OFF = H1_OFF + H1_BYTES, H2_BYTES = 256 * NSL * 8 * 16;
constexpr int WS_BYTES = H2_OFF + H2_BYTES;
constexpr int AUX_SC1 = 16;
constexpr unsigned SPIN_CAP = 1u << 18;
#define WD_WAIT 16
#define EPOCH_SCALAR 1
#define NOPOLL1 0
#define NOPOLL2 0
#define SLEEP1 0
#define SLEEP2 0
#define MAPSAME 0
#define STAMP(k) do {} while (0)
#define STAMP_FLUSH() do {} while (0)

__device__ __forceinline__ unsigned f2bf(float f) {
  __hip_bfloat16 b = __float2bfloat16(f);
  return (unsigned)__builtin_bit_cast(unsigned short, b);
}
__device__ __forceinline__ float bf2f(unsigned b) { return __builtin_bit_cast(float, b << 16); }
__device__ __forceinline__ float rbf(float f) { return bf2f(f2bf(f)); }
__device__ __forceinline__ float asf(unsigned u) { return __builtin_bit_cast(float, u); }
__device__ __forceinline__ unsigned asu(float f) { return __builtin_bit_cast(unsigned, f); }
__device__ __forceinline__ f32x4 mfma32(v4u a, v4u b, f32x4 c) {
  return __builtin_amdgcn_mfma_f32_16x16x32_bf16(__builtin_bit_cast(bf16x8, a), __builtin_bit_cast(bf16x8, b), c, 0, 0, 0);
}
__device__ __forceinline__ float hval(float g, float u) {
  const float gs = rbf(g), us = rbf(u);
  const float sg = rbf(1.0f / (1.0f + __expf(-gs)));
  return sg * us;
}

__global__ void __launch_bounds__(NTHR, 3)
glu_fused(const unsigned short* __restrict__ x, const unsigned short* __restrict__ wg, const unsigned short* __restrict__ wu,
          const unsigned short* __restrict__ wd, unsigned* __restrict__ out, unsigned char* ws,
          const unsigned long long* __restrict__ epoch_in) {
  __shared__ __attribute__((aligned(16))) float red[16 * 256];
  __shared__ __attribute__((aligned(16))) float red2[4 * 512];
  __shared__ __attribute__((aligned(16))) unsigned short hs[4 * 64];
  const int id = blockIdx.x;
  const int s = MAPSAME ? (id & 7) + 8 * (id >> 5) : id >> 2, q = MAPSAME ? (id >> 3) & 3 : id & 3;
  const int tid = threadIdx.x, l = tid & 63, w = __builtin_amdgcn_readfirstlane(tid >> 6);
  const int np = l & 15, kg = l >> 4;
  const __amdgpu_buffer_rsrc_t rs = __builtin_amdgcn_make_buffer_rsrc((void*)ws, 0, WS_BYTES, 0x00020000);
  asm volatile("" :: "s"(x), "s"(wg), "s"(wu), "s"(wd), "s"(out), "s"(ws), "s"(epoch_in));
  STAMP(0);
  const unsigned long long ep = *epoch_in;
  unsigned tagA, tagB;
  unsigned long long ep1;
  if (w < 8) {
    const int m = q & 1, kh = q >> 1;
    const int r0 = 512 * kh + 64 * w + 32 * (np >> 3) + 8 * kg;
    const unsigned short* p1 = (m ? wu : wg) + (size_t)r0 * F + 64 * s + 8 * (np & 7);
    v4u R[8];
#pragma unroll
    for (int i = 0; i < 8; ++i) R[i] = *(const v4u*)(p1 + (size_t)i * F);
    const v4u xa = *(const v4u*)(x + (np & 3) * D + r0);
    __builtin_amdgcn_sched_barrier(0);
    __builtin_amdgcn_s_barrier();
    __builtin_amdgcn_sched_barrier(0);
    STAMP(1);
    f32x4 acc[4];
#pragma unroll
    for (int jj = 0; jj < 4; ++jj) acc[jj] = f32x4{0.f, 0.f, 0.f, 0.f};
#pragma unroll
    for (int rr = 0; rr < 2; ++rr) {
      const bool on = ((np >> 2) & 1) == rr;
      v4u a;
#pragma unroll
      for (int e = 0; e < 4; ++e) a[e] = on ? xa[e] : 0u;
#pragma unroll
      for (int jj = 0; jj < 4; ++jj) {
        const int d = 2 * rr + (jj >> 1);
        const unsigned sel = (jj & 1) ? 0x07060302u : 0x05040100u;
        v4u b;
#pragma unroll
        for (int pp = 0; pp < 4; ++pp) b[pp] = __builtin_amdgcn_perm(R[2 * pp + 1][d], R[2 * pp][d], sel);
        acc[jj] = mfma32(a, b, acc[jj]);
      }
    }
    if ((np >> 3) == (kg >> 1)) {
      float* dst = &red[((w * 2 + (kg >> 1)) * 64 + 8 * (np & 7) + 4 * (kg & 1)) * 4];
#pragma unroll
      for (int jj = 0; jj < 4; ++jj) *(f32x4*)(dst + 4 * jj) = acc[jj];
    }
    ep1 = ep + 0x9E3779B97F4A7C15ull;
    tagA = __builtin_amdgcn_readfirstlane((unsigned)ep1); tagB = __builtin_amdgcn_readfirstlane((unsigned)(ep1 >> 32));
    STAMP(2);
    __syncthreads();
    STAMP(3);
    if (w < 2) {
      f32x2 mine = {0.f, 0.f};
#pragma unroll
      for (int p = 0; p < 16; ++p) mine += *(const f32x2*)&red[p * 256 + tid * 2];
      const v4u pub = {asu(mine[0]), tagA, asu(mine[1]), tagB};
      __builtin_amdgcn_raw_buffer_store_b128(pub, rs, H1_OFF + ((s * 4 + q) * 128 + tid) * 16, 0, AUX_SC1);
      v4u o[3];
      bool ok[3] = {false, false, false};
      for (unsigned spins = 0; spins < SPIN_CAP; ++spins) {
        asm volatile("" ::: "memory");
#pragma unroll
        for (int k = 0; k < 3; ++k)
          if (!ok[k]) o[k] = __builtin_amdgcn_raw_buffer_load_b128(rs, H1_OFF + ((s * 4 + (q ^ (k == 0 ? 2 : k == 1 ? 1 : 3))) * 128 + tid) * 16, 0, AUX_SC1);
#pragma unroll
        for (int k = 0; k < 3; ++k) ok[k] = o[k][1] == tagA && o[k][3] == tagB;
        if (NOPOLL1 || __all(ok[0] && ok[1] && ok[2])) break;
        if (SLEEP1) __builtin_amdgcn_s_sleep(SLEEP1);
      }
      const float a0 = mine[0] + asf(o[0][0]), a1 = mine[1] + asf(o[0][2]);
      const float c0 = asf(o[1][0]) + asf(o[2][0]), c1 = asf(o[1][2]) + asf(o[2][2]);
      const float h0 = m ? hval(c0, a0) : hval(a0, c0);
      const float h1 = m ? hval(c1, a1) : hval(a1, c1);
      const int col = tid >> 1, b = (tid & 1) * 2;
      hs[b * 64 + col] = (unsigned short)f2bf(h0);
      hs[(b + 1) * 64 + col] = (unsigned short)f2bf(h1);
      STAMP(4);
    }
    __syncthreads();
    __syncthreads();
  } else {
    const int v = w - 8, ch = v & 1, kh = v >> 1;
    const unsigned short* p2 = wd + (size_t)(64 * s + 32 * kh + 8 * kg) * D + 256 * q + 128 * ch + 8 * np;
    __builtin_amdgcn_s_barrier();
    v4u R[8];
#pragma unroll
    for (int i = 0; i < 8; ++i) R[i] = *(const v4u*)(p2 + (size_t)i * D);
    __builtin_amdgcn_sched_barrier(0);
    STAMP(1);
    __builtin_amdgcn_s_barrier();
    STAMP(2);
    __syncthreads();
    STAMP(4);
    const v4u ha = *(const v4u*)&hs[(np & 3) * 64 + 32 * kh + 8 * kg];
    f32x4 acc0 = {0.f, 0.f, 0.f, 0.f}, acc1 = acc0;
#pragma unroll
    for (int r = 0; r < 4; ++r) {
      const bool on = (np >> 2) == r;
      v4u a;
#pragma unroll
      for (int e = 0; e < 4; ++e) a[e] = on ? ha[e] : 0u;
      v4u b0, b1;
#pragma unroll
      for (int pp = 0; pp < 4; ++pp) {
        b0[pp] = __builtin_amdgcn_perm(R[2 * pp + 1][r], R[2 * pp][r], 0x05040100u);
        b1[pp] = __builtin_amdgcn_perm(R[2 * pp + 1][r], R[2 * pp][r], 0x07060302u);
      }
      acc0 = mfma32(a, b0, acc0);
      acc1 = mfma32(a, b1, acc1);
    }
    const int c0 = np * 8 + 2 * kg;
#pragma unroll
    for (int b = 0; b < 4; ++b) {
      f32x2 vv = {acc0[b], acc1[b]};
      *(f32x2*)&red2[v * 512 + b * 128 + c0] = vv;
    }
    __syncthreads();
    STAMP(5);
    STAMP_FLUSH();
    return;
  }
  {
    const int b = tid >> 7, cpl = tid & 127, c2 = cpl >> 6;
    f32x2 sum = *(const f32x2*)&red2[c2 * 512 + b * 128 + 2 * (cpl & 63)] + *(const f32x2*)&red2[(2 + c2) * 512 + b * 128 + 2 * (cpl & 63)];
    const int r = b * 64 + 16 * q + (cpl >> 3);
    const v4u pub = {asu(sum[0]), tagA, asu(sum[1]), tagB};
    __builtin_amdgcn_raw_buffer_store_b128(pub, rs, H2_OFF + ((r * NSL + s) * 8 + (cpl & 7)) * 16, 0, AUX_SC1);
  }
  STAMP(5);
  if (w == 0 || (w == 1 && id < 32)) {
    const int r = id + NWG * w;
    v4u o[7];
    bool ok[7];
#pragma unroll
    for (int k = 0; k < 7; ++k) ok[k] = false;
    for (unsigned spins = 0; spins < SPIN_CAP; ++spins) {
      asm volatile("" ::: "memory");
#pragma unroll
      for (int k = 0; k < 7; ++k)
        if (!ok[k]) o[k] = __builtin_amdgcn_raw_buffer_load_b128(rs, H2_OFF + (r * NSL * 8 + k * 64 + l) * 16, 0, AUX_SC1);
      bool all = true;
#pragma unroll
      for (int k = 0; k < 7; ++k) { ok[k] = o[k][1] == tagA && o[k][3] == tagB; all = all && ok[k]; }
      if (NOPOLL2 || __all(all)) break;
      if (SLEEP2) __builtin_amdgcn_s_sleep(SLEEP2);
    }
    float s0 = 0.f, s1 = 0.f;
#pragma unroll
    for (int k = 0; k < 7; ++k) { s0 += asf(o[k][0]); s1 += asf(o[k][2]); }
#pragma unroll
    for (int mm = 8; mm < 64; mm <<= 1) { s0 += __shfl_xor(s0, mm); s1 += __shfl_xor(s1, mm); }
    STAMP(6);
    if (l < 8) out[r * 8 + l] = f2bf(s0) | (f2bf(s1) << 16);
    if (r == 0 && l == 0) __hip_atomic_store((GAS unsigned long long*)ws, ep1, RLX_AGENT);
  }
  STAMP_FLUSH();
}

extern "C" void kernel_launch(void* const* d_in, const int* in_sizes, int n_in, void* d_out, int out_size, void* d_ws, size_t ws_size,
                              hipStream_t stream) {
  glu_fused<<<NWG, NTHR, 0, stream>>>((const unsigned short*)d_in[0], (const unsigned short*)d_in[1], (const unsigned short*)d_in[2],
                                      (const unsigned short*)d_in[3], (unsigned*)d_out, (unsigned char*)d_ws, (const unsigned long long*)d_ws);
}
```

```cpp
#include <hip/hip_runtime.h>
#include <hip/hip_bf16.h>
#include <stdint.h>

typedef unsigned v4u __attribute__((ext_vector_type(4)));
typedef float f32x4 __attribute__((ext_vector_type(4)));
typedef float f32x2 __attribute__((ext_vector_type(2)));
typedef short bf16x8 __attribute__((ext_vector_type(8)));
#define GAS __attribute__((address_space(1)))
#define RLX_AGENT __ATOMIC_RELAXED, __HIP_MEMORY_SCOPE_AGENT

constexpr int D = 1024, F = 3584;
constexpr int NSL = 56, NWG = 224, NTHR = 768;
constexpr int H1_OFF = 4096, H1_BYTES = NWG * 128 * 16;
constexpr int H2_OFF = H1_OFF + H1_BYTES, H2_BYTES = 256 * NSL * 8 * 16;
constexpr int WS_BYTES = H2_OFF + H2_BYTES;
constexpr int AUX_SC1 = 16;
constexpr int RSRC3 = 0x00020000;
constexpr unsigned SPIN_CAP = 1u << 18;
constexpr int PSTR = 260;
constexpr unsigned SEL_LO = 0x05040100u, SEL_HI = 0x07060302u;
constexpr int QP_X1 = 0xB1, QP_X2 = 0x4E, ROW_ROR8 = 0x128;

__device__ __forceinline__ float asf(unsigned u) { return __builtin_bit_cast(float, u); }
__device__ __forceinline__ unsigned asu(float f) { return __builtin_bit_cast(unsigned, f); }
__device__ __forceinline__ unsigned pk_bf16(float lo, float hi) {
  typedef __bf16 bf2 __attribute__((ext_vector_type(2)));
  f32x2 v = {lo, hi};
  return __builtin_bit_cast(unsigned, __builtin_convertvector(v, bf2));
}
__device__ __forceinline__ f32x4 mfma32(v4u a, v4u b, f32x4 c) {
  return __builtin_amdgcn_mfma_f32_16x16x32_bf16(__builtin_bit_cast(bf16x8, a), __builtin_bit_cast(bf16x8, b), c, 0, 0, 0);
}
template <int CTRL> __device__ __forceinline__ float dpp(float v) {
  return asf(__builtin_amdgcn_update_dpp(0u, asu(v), CTRL, 0xf, 0xf, true));
}
__device__ __forceinline__ void transpose8(const v4u (&R)[8], v4u (&B)[8]) {
#pragma unroll
  for (int pp = 0; pp < 4; ++pp) {
#pragma unroll
    for (int d = 0; d < 4; ++d) {
      B[2 * d][pp] = __builtin_amdgcn_perm(R[2 * pp + 1][d], R[2 * pp][d], SEL_LO);
      B[2 * d + 1][pp] = __builtin_amdgcn_perm(R[2 * pp + 1][d], R[2 * pp][d], SEL_HI);
    }
    __builtin_amdgcn_sched_barrier(0);
  }
}

__global__ void __launch_bounds__(NTHR, 6)
glu_fused(const unsigned short* __restrict__ x, const unsigned short* __restrict__ wg, const unsigned short* __restrict__ wu,
          const unsigned short* __restrict__ wd, unsigned short* __restrict__ out, unsigned char* ws,
          const unsigned long long* __restrict__ epoch_in) {
  __shared__ __attribute__((aligned(16))) float red[16 * PSTR];
  __shared__ __attribute__((aligned(16))) float red2[4 * 512];
  __shared__ __attribute__((aligned(16))) unsigned short hs[4 * 64 + 64];
  const int id = blockIdx.x, s = id >> 2, q = id & 3;
  const int tid = threadIdx.x, l = tid & 63, w = __builtin_amdgcn_readfirstlane(tid >> 6);
  const int np = l & 15, kg = l >> 4;
  asm volatile("" :: "s"(x), "s"(wg), "s"(wu), "s"(wd), "s"(out), "s"(ws), "s"(epoch_in));
  const __amdgpu_buffer_rsrc_t rs = __builtin_amdgcn_make_buffer_rsrc((void*)ws, 0, WS_BYTES, RSRC3);

  if (w < 8) {
    const unsigned long long ep1 = *epoch_in + 0x9E3779B97F4A7C15ull;
    const unsigned tagA = (unsigned)ep1, tagB = (unsigned)(ep1 >> 32);
    const int m = q & 1, kh = q >> 1;
    const int r0 = 512 * kh + 64 * w + 32 * (np >> 3) + 8 * kg;
    const __amdgpu_buffer_rsrc_t rw = __builtin_amdgcn_make_buffer_rsrc((void*)(m ? wu : wg), 0, D * F * 2, RSRC3);
    const __amdgpu_buffer_rsrc_t rx = __builtin_amdgcn_make_buffer_rsrc((void*)x, 0, 4 * D * 2, RSRC3);
    const int voff = r0 * (F * 2) + (64 * s + 8 * (np & 7)) * 2;
    const v4u xa = __builtin_amdgcn_raw_buffer_load_b128(rx, ((np & 3) * D + r0) * 2, 0, 0);
    v4u R[8];
#pragma unroll
    for (int i = 0; i < 8; ++i) R[i] = __builtin_amdgcn_raw_buffer_load_b128(rw, voff, i * (F * 2), 0);
    __builtin_amdgcn_sched_barrier(0);
    __builtin_amdgcn_s_barrier();
    __builtin_amdgcn_sched_barrier(0);
    const int u = tid >> 2, p = tid & 3;
    int pub1 = H1_OFF + (id * 128 + u) * 16;
    int poff = H1_OFF + ((s * 4 + (q ^ (p == 1 ? 2 : p == 2 ? 1 : 3))) * 128 + u) * 16;
    int rsrc_off = ((4 * p) * PSTR + 2 * u) * 4;
    int hdst = (((u & 1) * 2 + p) * 64 + (u >> 1)) * 2;
    int wdst = ((w * 2 + (kg >> 1)) * PSTR + (8 * (np & 7) + 4 * (kg & 1)) * 4) * 4;
    int r2src = ((tid >> 8) * 512 + ((tid >> 1) & 127) * 4 + (tid & 1) * 2) * 4;
    int pub2 = H2_OFF + (((64 * q + (tid >> 3)) * NSL + s) * 8 + (tid & 7)) * 16;
    int gsrc = H2_OFF + ((id + NWG * w) * NSL * 8 + l) * 16;
    asm volatile("" : "+v"(pub1), "+v"(poff), "+v"(rsrc_off), "+v"(hdst), "+v"(wdst), "+v"(r2src), "+v"(pub2), "+v"(gsrc));
    const bool on0 = ((np >> 2) & 1) == 0;
    v4u a0, a1;
#pragma unroll
    for (int e = 0; e < 4; ++e) { a0[e] = on0 ? xa[e] : 0u; a1[e] = on0 ? 0u : xa[e]; }
    v4u B[8];
    transpose8(R, B);
    f32x4 acc[4];
#pragma unroll
    for (int jj = 0; jj < 4; ++jj) acc[jj] = mfma32(a0, B[jj], f32x4{0.f, 0.f, 0.f, 0.f});
#pragma unroll
    for (int jj = 0; jj < 4; ++jj) acc[jj] = mfma32(a1, B[4 + jj], acc[jj]);
    if ((np >> 3) == (kg >> 1)) {
      float* dst = (float*)((char*)red + wdst);
#pragma unroll
      for (int jj = 0; jj < 4; ++jj) *(f32x4*)(dst + 4 * jj) = acc[jj];
    }
    __syncthreads();
    {
      const float* src = (const float*)((const char*)red + rsrc_off);
      const f32x2 v = (*(const f32x2*)src + *(const f32x2*)(src + PSTR)) + (*(const f32x2*)(src + 2 * PSTR) + *(const f32x2*)(src + 3 * PSTR));
      float v0 = v[0], v1 = v[1];
      v0 += dpp<QP_X1>(v0); v1 += dpp<QP_X1>(v1);
      v0 += dpp<QP_X2>(v0); v1 += dpp<QP_X2>(v1);
      v4u o = {asu(v0), tagA, asu(v1), tagB};
      if (p == 0) __builtin_amdgcn_raw_buffer_store_b128(o, rs, pub1, 0, AUX_SC1);
      bool ok = p == 0;
      __builtin_amdgcn_s_sleep(8);
      for (unsigned spins = 0; spins < SPIN_CAP; ++spins) {
        asm volatile("" ::: "memory");
        if (!ok) o = __builtin_amdgcn_raw_buffer_load_b128(rs, poff, 0, AUX_SC1);
        ok = o[1] == tagA && o[3] == tagB;
        if (__all(ok)) break;
      }
      float e0 = asf(o[0]), e1 = asf(o[2]);
      e0 += dpp<QP_X1>(e0); e1 += dpp<QP_X1>(e1);
      const float f0 = dpp<QP_X2>(e0), f1 = dpp<QP_X2>(e1);
      const float mine = (p & 1) ? e1 : e0, other = (p & 1) ? f1 : f0;
      const bool mine_is_gate = (m == 0) == (p < 2);
      const float g = mine_is_gate ? mine : other, uu = mine_is_gate ? other : mine;
      const unsigned gu = pk_bf16(g, uu);
      const float gs = asf(gu << 16), us = asf(gu & 0xffff0000u);
      const float sg = __builtin_amdgcn_rcpf(1.0f + __expf(-gs));
      const float sgs = asf(pk_bf16(sg, sg) << 16);
      const float hv = sgs * us;
      if (p < 2) *(unsigned short*)((char*)hs + hdst) = (unsigned short)pk_bf16(hv, hv);
    }
    __syncthreads();
    __syncthreads();
    {
      const f32x2 sum = *(const f32x2*)((const char*)red2 + r2src) + *(const f32x2*)((const char*)red2 + r2src + 2 * 512 * 4);
      const v4u pub = {asu(sum[0]), tagA, asu(sum[1]), tagB};
      __builtin_amdgcn_raw_buffer_store_b128(pub, rs, pub2, 0, AUX_SC1);
    }
    if (w == 0 || (w == 1 && id < 32)) {
      const int r = id + NWG * w;
      __builtin_amdgcn_s_sleep(10);
      v4u o[7];
      bool ok[7];
#pragma unroll
      for (int k = 0; k < 7; ++k) ok[k] = false;
      for (unsigned spins = 0; spins < SPIN_CAP; ++spins) {
        asm volatile("" ::: "memory");
#pragma unroll
        for (int k = 0; k < 7; ++k)
          if (!ok[k]) o[k] = __builtin_amdgcn_raw_buffer_load_b128(rs, gsrc, k * 1024, AUX_SC1);
        bool all = true;
#pragma unroll
        for (int k = 0; k < 7; ++k) { ok[k] = o[k][1] == tagA && o[k][3] == tagB; all = all && ok[k]; }
        if (__all(all)) break;
      }
      float s0 = ((asf(o[0][0]) + asf(o[1][0])) + (asf(o[2][0]) + asf(o[3][0]))) + ((asf(o[4][0]) + asf(o[5][0])) + asf(o[6][0]));
      float s1 = ((asf(o[0][2]) + asf(o[1][2])) + (asf(o[2][2]) + asf(o[3][2]))) + ((asf(o[4][2]) + asf(o[5][2])) + asf(o[6][2]));
      s0 += dpp<ROW_ROR8>(s0); s1 += dpp<ROW_ROR8>(s1);
      {
        auto t0 = __builtin_amdgcn_permlane16_swap(asu(s0), asu(s0), false, false); s0 = asf(t0[0]) + asf(t0[1]);
        auto t1 = __builtin_amdgcn_permlane16_swap(asu(s1), asu(s1), false, false); s1 = asf(t1[0]) + asf(t1[1]);
        auto t2 = __builtin_amdgcn_permlane32_swap(asu(s0), asu(s0), false, false); s0 = asf(t2[0]) + asf(t2[1]);
        auto t3 = __builtin_amdgcn_permlane32_swap(asu(s1), asu(s1), false, false); s1 = asf(t3[0]) + asf(t3[1]);
      }
      if (l < 8) {
        const unsigned pk = pk_bf16(s0, s1);
        const int col = 4 * r + (l >> 1), b = (l & 1) * 2;
        __hip_atomic_store((GAS unsigned short*)(out + b * D + col), (unsigned short)pk, RLX_AGENT);
        __hip_atomic_store((GAS unsigned short*)(out + (b + 1) * D + col), (unsigned short)(pk >> 16), RLX_AGENT);
      }
      if (r == 0 && l == 0) __hip_atomic_store((GAS unsigned long long*)ws, ep1, RLX_AGENT);
    }
  } else {
    const int v = w - 8, ch = v & 1, kh = v >> 1;
    const __amdgpu_buffer_rsrc_t rw = __builtin_amdgcn_make_buffer_rsrc((void*)wd, 0, D * F * 2, RSRC3);
    const __amdgpu_buffer_rsrc_t ro = __builtin_amdgcn_make_buffer_rsrc((void*)out, 0, 4 * D * 2, RSRC3);
    const int voff = (64 * s + 32 * kh + 8 * kg) * (D * 2) + (256 * q + 128 * ch + 8 * np) * 2;
    if (v == 0 && l < 32) ((unsigned*)&hs[256])[l] = 0u;
    const v4u t0 = __builtin_amdgcn_raw_buffer_load_b128(rs, 256 + (v & 1) * (2 << 20), 0, 0);
    const unsigned t1 = __builtin_amdgcn_raw_buffer_load_b32(ro, (l & 1) * 4096 + (v & 1) * 2048, 0, 0);
    __builtin_amdgcn_s_barrier();
    v4u R[8];
#pragma unroll
    for (int i = 0; i < 8; ++i) R[i] = __builtin_amdgcn_raw_buffer_load_b128(rw, voff, i * (D * 2), 0);
    __builtin_amdgcn_sched_barrier(0);
    __builtin_amdgcn_s_barrier();
    __builtin_amdgcn_sched_barrier(0);
    v4u B[8];
    transpose8(R, B);
    int ap[4];
#pragma unroll
    for (int r = 0; r < 4; ++r) ap[r] = ((np >> 2) == r) ? ((np & 3) * 64 + 32 * kh + 8 * kg) * 2 : 512;
#pragma unroll
    for (int j = 0; j < 8; ++j) asm volatile("" : "+v"(B[j]));
    asm volatile("" :: "v"(t0), "v"(t1));
    int wdst2 = (v * 512 + (np * 8 + 2 * kg) * 4) * 4;
    asm volatile("" : "+v"(wdst2), "+v"(ap[0]), "+v"(ap[1]), "+v"(ap[2]), "+v"(ap[3]));
    __syncthreads();
    v4u a[4];
#pragma unroll
    for (int r = 0; r < 4; ++r) a[r] = *(const v4u*)((const char*)hs + ap[r]);
    __builtin_amdgcn_sched_barrier(0);
    f32x4 acc0 = {0.f, 0.f, 0.f, 0.f}, acc1 = acc0;
#pragma unroll
    for (int r = 0; r < 4; ++r) {
      acc0 = mfma32(a[r], B[2 * r], acc0);
      acc1 = mfma32(a[r], B[2 * r + 1], acc1);
    }
    *(f32x4*)((char*)red2 + wdst2) = acc0;
    *(f32x4*)((char*)red2 + wdst2 + 16) = acc1;
    __syncthreads();
  }
}

extern "C" void kernel_launch(void* const* d_in, const int* in_sizes, int n_in, void* d_out, int out_size, void* d_ws, size_t ws_size,
                              hipStream_t stream) {
  glu_fused<<<NWG, NTHR, 0, stream>>>((const unsigned short*)d_in[0], (const unsigned short*)d_in[1], (const unsigned short*)d_in[2],
                                      (const unsigned short*)d_in[3], (unsigned short*)d_out, (unsigned char*)d_ws,
                                      (const unsigned long long*)d_ws);
}
```
